```python
import torch

def _softmax(x, axis=-1):
    return torch.softmax(x, dim=axis)

def _gelu(x):
    return torch.nn.functional.gelu(x, approximate='tanh')

def _silu(x):
    return torch.nn.functional.silu(x)

def _sigmoid(x):
    return torch.sigmoid(x)

def _layernorm(x, axis=-1, eps=1e-05):
    m = x.mean(dim=axis, keepdim=True)
    v = x.var(dim=axis, keepdim=True, unbiased=False)
    return (x - m) / torch.sqrt(v + eps)

def _scatter_add(tgt, idx, src, axis=0):
    return tgt.clone().index_add_(axis, idx, src)

def _segment_sum(src, seg, n):
    out = torch.zeros((n,) + src.shape[1:], dtype=src.dtype)
    return out.index_add_(0, seg, src)

def _pad(x, pw):
    flat = tuple((v for pair in reversed(pw) for v in pair))
    return torch.nn.functional.pad(x, flat)
kernel_name = 'adaLN_cond__gqa_block_cached__swiglu_ffn__decode'

def setup_inputs():

    _w = lambda a: (lambda t: t.to(torch.bfloat16) if t.is_floating_point() else t)(torch.as_tensor(a))

    def _A_setup():
        B, Sq, D = (1, 1, 512)
        Hq, Hkv, Sk, Dh = (32, 8, 65536, 128)
        assert Hq % Hkv == 0, (Hq, Hkv)
        return {'x': _w((lambda *s: torch.randn(*s, dtype=torch.bfloat16) * 0.05)(B, Sq, D)), 'k_cache': _w((lambda *s: torch.randn(*s, dtype=torch.bfloat16) * 3.2)(B, Hkv, Sk, Dh)), 'v_cache': _w((lambda *s: torch.randn(*s, dtype=torch.bfloat16) * 0.4)(B, Hkv, Sk, Dh)), 'wq': _w((lambda *s: torch.randn(*s, dtype=torch.bfloat16) * 3.2)(D, Hq * Dh) * D ** (-0.5)), 'wo': _w((lambda *s: torch.randn(*s, dtype=torch.bfloat16) * 0.4)(Hq * Dh, D) * (Hq * Dh) ** (-0.5))}
    _A_in = _A_setup()

    def _B_setup():
        B, S, D, F = (1, 1, 512, 1792)
        return {'x': _w((lambda *s: torch.randn(*s, dtype=torch.bfloat16) * 0.05)(B, S, D)), 'w_gate': _w((lambda *s: torch.randn(*s, dtype=torch.bfloat16) * 0.05)(D, F)), 'w_up': _w((lambda *s: torch.randn(*s, dtype=torch.bfloat16) * 0.05)(D, F)), 'w_down': _w((lambda *s: torch.randn(*s, dtype=torch.bfloat16) * 0.05)(F, D))}
    _B_in = _B_setup()
    return {'A_x': _A_in['x'], 'A_k_cache': _A_in['k_cache'], 'A_v_cache': _A_in['v_cache'], 'A_wq': _A_in['wq'], 'A_wo': _A_in['wo'], 'B_x': _B_in['x'], 'B_w_gate': _B_in['w_gate'], 'B_w_up': _B_in['w_up'], 'B_w_down': _B_in['w_down'], 'c': _w((lambda *s: torch.randn(*s, dtype=torch.bfloat16) * 0.2)(1, 512) * 8.0), 'w_mod': _w((lambda *s: torch.randn(*s, dtype=torch.bfloat16) * 0.2)(512, 6 * 512) * 512 ** (-0.5) * 8.0)}

def reference(A_x, A_k_cache, A_v_cache, A_wq, A_wo, B_x, B_w_gate, B_w_up, B_w_down, c, w_mod):

    def _A_ref(x, k_cache, v_cache, wq, wo):
        B, Sq, D = x.shape
        Hq, Dh = (32, 128)
        Hkv = k_cache.shape[1]
        rep = Hq // Hkv
        q = (lambda x, *d: x.permute(*d))((x @ wq).reshape(B, Sq, Hq, Dh), 0, 2, 1, 3)
        k = (lambda x, n, axis: torch.repeat_interleave(x, n, dim=axis))(k_cache, rep, axis=1)
        v = (lambda x, n, axis: torch.repeat_interleave(x, n, dim=axis))(v_cache, rep, axis=1)
        scores = torch.einsum('bhqd,bhkd->bhqk', q, k) * Dh ** (-0.5)
        p = _softmax(scores, axis=-1)
        o = torch.einsum('bhqk,bhkd->bhqd', p, v)
        return (lambda x, *d: x.permute(*d))(o, 0, 2, 1, 3).reshape(B, Sq, Hq * Dh) @ wo

    def _B_ref(x, w_gate, w_up, w_down):
        g = _silu(x @ w_gate)
        u = x @ w_up
        return g * u @ w_down
    x = A_x
    mod = (_silu(c) @ w_mod).reshape(c.shape[0], 6, 1, -1)
    sa, ca, ga, sb, cb, gb = (mod[:, 0].clone(), mod[:, 1].clone(), mod[:, 2].clone(), mod[:, 3].clone(), mod[:, 4].clone(), mod[:, 5].clone())
    h = _layernorm(x, axis=-1) * (1 + ca) + sa
    x = x + ga * _A_ref(h, A_k_cache, A_v_cache, A_wq, A_wo)
    h = _layernorm(x, axis=-1) * (1 + cb) + sb
    x = x + gb * _B_ref(h, B_w_gate, B_w_up, B_w_down)
    return x
```

```cpp
#include <hip/hip_runtime.h>
#include <hip/hip_bf16.h>
#include <stdint.h>
#define MIMIC 0
typedef unsigned short u16;
constexpr int D = 512, HQ = 32, HKV = 8, SK = 65536, DH = 128, F = 1792, NSPLIT = 64;
__device__ __forceinline__ float bf2f(u16 b) { return __uint_as_float((unsigned)b << 16); }
__device__ __forceinline__ u16 f2bf(float f) { unsigned u = __float_as_uint(f); u += 0x7FFFu + ((u >> 16) & 1u); return (u16)(u >> 16); }
__device__ __forceinline__ float rb(float f) {
  return f;
}
__device__ float block_sum(float v, float* red) {
  for (int o = 32; o; o >>= 1) v += __shfl_xor(v, o);
  __syncthreads();
  if ((threadIdx.x & 63) == 0) red[threadIdx.x >> 6] = v;
  __syncthreads();
  float s = 0; for (int i = 0; i < (int)(blockDim.x >> 6); ++i) s += red[i];
  return s;
}
__global__ void k_mod(const u16* c, const u16* w, float* mod) {
  __shared__ float sc[D];
  for (int i = threadIdx.x; i < D; i += blockDim.x) { float v = bf2f(c[i]); sc[i] = rb(v / (1.f + expf(-v))); }
  __syncthreads();
  int n = blockIdx.x * blockDim.x + threadIdx.x;
  float a = 0; for (int k = 0; k < D; ++k) a += sc[k] * bf2f(w[(size_t)k * 3072 + n]);
  mod[n] = rb(a);
}
__device__ void ln_mod(const float* x, const float* shift, const float* scale, float* h, float* red) {
  float v = x[threadIdx.x];
  float mean = block_sum(v, red) / D;
  float d = v - mean;
  float var = block_sum(d * d, red) / D;
  float m = rb(mean), vr = rb(var);
  float den = rb(sqrtf(rb(vr + 1e-5f)));
  float n = rb(rb(v - m) / den);
  h[threadIdx.x] = rb(rb(n * rb(1.f + scale[threadIdx.x])) + shift[threadIdx.x]);
  __syncthreads();
}
__global__ void k_q(const u16* x, const float* mod, const u16* wq, float* q) {
  __shared__ float xs[D], h[D], red[16];
  xs[threadIdx.x] = bf2f(x[threadIdx.x]); __syncthreads();
  ln_mod(xs, mod, mod + D, h, red);
  int n = blockIdx.x * blockDim.x + threadIdx.x;
  float a = 0; for (int k = 0; k < D; ++k) a += h[k] * bf2f(wq[(size_t)k * 4096 + n]);
  q[n] = rb(a);
}
__global__ void k_attn(const float* q, const u16* K, const u16* V, float* pm, float* pl, float* po) {
  __shared__ float qs[4][DH], ps[4][1024], red[16];
  int s = blockIdx.x, h = blockIdx.y, t = threadIdx.x;
  constexpr int KPS = SK / NSPLIT;
  for (int i = t; i < 512; i += 256) qs[i >> 7][i & 127] = q[h * 512 + i];
  __syncthreads();
  float mx[4] = {-INFINITY, -INFINITY, -INFINITY, -INFINITY};
  for (int j = t; j < KPS; j += 256) {
    const uint4* kr = (const uint4*)(K + ((size_t)h * SK + s * KPS + j) * DH);
    float a[4] = {0, 0, 0, 0};
    for (int cch = 0; cch < 16; ++cch) { uint4 u = kr[cch]; unsigned w[4] = {u.x, u.y, u.z, u.w};
      for (int e = 0; e < 4; ++e) { float lo = __uint_as_float(w[e] << 16), hi = __uint_as_float(w[e] & 0xffff0000u);
        for (int g = 0; g < 4; ++g) a[g] += lo * qs[g][cch * 8 + 2 * e] + hi * qs[g][cch * 8 + 2 * e + 1]; } }
    for (int g = 0; g < 4; ++g) { float sc = rb(rb(a[g]) * 0.08838834764831845f); ps[g][j] = sc; mx[g] = fmaxf(mx[g], sc); }
  }
  float l[4];
  for (int g = 0; g < 4; ++g) {
    float v = mx[g]; for (int o = 32; o; o >>= 1) v = fmaxf(v, __shfl_xor(v, o));
    __syncthreads(); if ((t & 63) == 0) red[t >> 6] = v; __syncthreads();
    mx[g] = fmaxf(fmaxf(red[0], red[1]), fmaxf(red[2], red[3]));
    float sum = 0; for (int j = t; j < KPS; j += 256) { float p = expf(ps[g][j] - mx[g]); ps[g][j] = p; sum += p; }
    l[g] = block_sum(sum, red);
  }
  __syncthreads();
  int d = t & 127, g2 = t >> 7; float o0 = 0, o1 = 0;
  for (int j = 0; j < KPS; ++j) { float v = bf2f(V[((size_t)h * SK + s * KPS + j) * DH + d]); o0 += ps[2 * g2][j] * v; o1 += ps[2 * g2 + 1][j] * v; }
  po[((h * 4 + 2 * g2) * NSPLIT + s) * DH + d] = o0; po[((h * 4 + 2 * g2 + 1) * NSPLIT + s) * DH + d] = o1;
  if (t < 4) { pm[(h * 4 + t) * NSPLIT + s] = mx[t]; pl[(h * 4 + t) * NSPLIT + s] = l[t]; }
}
__global__ void k_comb(const float* pm, const float* pl, const float* po, float* o) {
  int hq = blockIdx.x, d = threadIdx.x;
  float M = -INFINITY; for (int s = 0; s < NSPLIT; ++s) M = fmaxf(M, pm[hq * NSPLIT + s]);
  float L = 0, a = 0; for (int s = 0; s < NSPLIT; ++s) { float w = expf(pm[hq * NSPLIT + s] - M); L += w * pl[hq * NSPLIT + s]; a += w * po[(hq * NSPLIT + s) * DH + d]; }
  o[hq * DH + d] = rb(a / L);
}
__global__ void k_wo(const float* o, const u16* wo, const u16* x, const float* mod, float* x1) {
  __shared__ float red[16];
  int n = blockIdx.x; float a = 0;
  for (int k = threadIdx.x; k < 4096; k += 256) a += o[k] * bf2f(wo[(size_t)k * D + n]);
  a = block_sum(a, red);
  if (threadIdx.x == 0) x1[n] = rb(bf2f(x[n]) + rb(mod[2 * D + n] * rb(a)));
}
__global__ void k_gu(const float* x1, const float* mod, const u16* wg, const u16* wu, float* act) {
  __shared__ float h[D], red[16];
  ln_mod(x1, mod + 3 * D, mod + 4 * D, h, red);
  int n = blockIdx.x * blockDim.x + threadIdx.x; if (n >= F) return;
  float g = 0, u = 0; for (int k = 0; k < D; ++k) { g += h[k] * bf2f(wg[(size_t)k * F + n]); u += h[k] * bf2f(wu[(size_t)k * F + n]); }
  g = rb(g); u = rb(u); g = rb(g / (1.f + expf(-g)));
  act[n] = rb(g * u);
}
__global__ void k_down(const float* act, const u16* wd, const float* x1, const float* mod, u16* out) {
  __shared__ float red[16];
  int n = blockIdx.x; float a = 0;
  for (int k = threadIdx.x; k < F; k += 256) a += act[k] * bf2f(wd[(size_t)k * D + n]);
  a = block_sum(a, red);
  if (threadIdx.x == 0) out[n] = f2bf(x1[n] + rb(mod[5 * D + n] * rb(a)));
}
extern "C" void kernel_launch(void* const* d_in, const int* in_sizes, int n_in, void* d_out, int out_size, void* d_ws, size_t ws_size, hipStream_t stream) {
  const u16 *x = (const u16*)d_in[0], *K = (const u16*)d_in[1], *V = (const u16*)d_in[2], *wq = (const u16*)d_in[3], *wo = (const u16*)d_in[4];
  const u16 *wg = (const u16*)d_in[6], *wu = (const u16*)d_in[7], *wd = (const u16*)d_in[8], *c = (const u16*)d_in[9], *wm = (const u16*)d_in[10];
  float* ws = (float*)d_ws;
  float *mod = ws, *q = ws + 4096, *o = ws + 8192, *x1 = ws + 12288, *act = ws + 13312, *pm = ws + 16384, *pl = ws + 16384 + 4096, *po = ws + 32768;
  k_mod<<<3072 / 64, 64, 0, stream>>>(c, wm, mod);
  k_q<<<4096 / 512, 512, 0, stream>>>(x, mod, wq, q);
  k_attn<<<dim3(NSPLIT, HKV), 256, 0, stream>>>(q, K, V, pm, pl, po);
  k_comb<<<HQ, DH, 0, stream>>>(pm, pl, po, o);
  k_wo<<<D, 256, 0, stream>>>(o, wo, x, mod, x1);
  k_gu<<<4, 512, 0, stream>>>(x1, mod, wg, wu, act);
  k_down<<<D, 256, 0, stream>>>(act, wd, x1, mod, (u16*)d_out);
}
```

```cpp
#include <hip/hip_runtime.h>
#include <stdint.h>

namespace {
typedef unsigned u32;
typedef unsigned short u16;
typedef unsigned long long u64;
typedef u32 v4u __attribute__((ext_vector_type(4)));
typedef u32 v2u __attribute__((ext_vector_type(2)));
typedef float v4f __attribute__((ext_vector_type(4)));
typedef float v2f __attribute__((ext_vector_type(2)));
typedef short v4s __attribute__((ext_vector_type(4)));
typedef __bf16 v8bf __attribute__((ext_vector_type(8)));
typedef __bf16 v2bf __attribute__((ext_vector_type(2)));
#define GAS __attribute__((address_space(1)))
#define LAS __attribute__((address_space(3)))
#define DEVI __device__ __forceinline__
#define INL __attribute__((always_inline))
#define KNAME "fused_decode_block"
#define CODE_BYTES 26368

#define PB 50
#define TC 580
#define DTC 80
#define GAP 430
#define NF0 6
#define HF0 520
#define HMIN 270
#define TRUN (116 * GAP / 16)
#define LAGU 6
#define TQ0 300
#define QSL 8
#define CLRAT 1
#define CLRAUX 16
#define QSLEEP 10
#define C1 150
#define C2 260
#define CX 355
#define C3 455
#define C4 560
#define D1 35
#define D2 48
#define D3 135
#define D4 230
#define TOUCH 1
#define PBITS 0
#define HURRY 4
#define TAILU 8
#define LFIRST 0
#define FWL 0
#define TF0 0
#define WARM 0
#define WARM0 1
#define REAN 11
#define TT0 200
#define TMB 260
#define SLACK 96
#define DX 45
#define D5 32
#define WSL 0
#define LSL 0
#define PSL 2

constexpr int D = 512, SK = 65536, DH = 128, F = 1792;
constexpr int NBLK = 256, NTHR = 320;
constexpr int NTF = 68, NTM = 62, NTQ = 58;
static_assert(2 * NTF + NTM + NTQ == 256 && 2 * NTQ == 116, "four CUs share the 256 blocks of a column");
constexpr int SLOT = 8192, L_S = 16 * SLOT, RING = L_S + NTF * 256;
constexpr int L_FULL = RING, L_DONE = RING + 16, L_CNTA = RING + 36, L_CNTB = RING + 36, L_QOK = RING + 40, L_GO = RING + 44, L_WOK = RING + 48;
constexpr int L_PUB = RING + 52, L_PRB = RING + 56, L_PUB2 = RING + 60, L_MRG = RING + 64, L_TAG = RING + 80, L_STAT = RING + 96;
constexpr int L_CNTM = RING + 4, L_CNTL = RING + 8, L_TREF = RING + 12, L_TEND = RING + 76, L_T0M = RING + 28 + 4, L_MX = RING + 128, L_LS = RING + 192, L_VECA = RING + 384, L_VECB = RING + 896, L_REDA = RING + 1920, L_REDB = RING + 2944, L_Q = RING + 3968;
constexpr int L_END = RING + 4992;
constexpr int LDS_BYTES = L_END;
static_assert(LDS_BYTES <= 163840, "LDS");
constexpr u32 PSTR = 8192 - 1024;
constexpr u32 SPIN_CAP = 1u << 18;
constexpr float LOG2E = 1.4426950408889634f, SCALE = 0.08838834764831845f, EPS = 1e-5f;
constexpr int W_E = 0, W_QS = 1024, W_T0 = 3072, W_RDY = 4096, W_GM = 8192, W_GQ = 24576, W_GP = 40960, W_ML = 57344, W_CLR = 81920;
constexpr int W_H1 = W_CLR, W_H2 = W_H1 + 524288, W_H3 = W_H2 + 524288, W_H4 = W_H3 + 224 * 2048, W_X1 = W_H4 + 64 * 7168;
constexpr int CLR_BYTES = 256 * 8192, W_END = W_CLR + CLR_BYTES;
static_assert(W_X1 + 4096 <= W_END, "cleared region covers every sentinel word");

struct Args {
  const u16 *x, *k, *v, *wq, *wo, *wg, *wu, *wd, *c, *wm;
  u16* out;
  unsigned char* ws;
};

DEVI float bflo(u32 p) { return __builtin_bit_cast(float, p << 16); }
DEVI float bfhi(u32 p) { return __builtin_bit_cast(float, p & 0xffff0000u); }
DEVI u32 pk(float lo, float hi) { return __builtin_bit_cast(u32, __builtin_convertvector(v2f{lo, hi}, v2bf)); }
DEVI float rb(float f) { return bflo(pk(f, f)); }
DEVI float dot2(u32 a, u32 b, float c) { return __builtin_amdgcn_fdot2_f32_bf16(__builtin_bit_cast(v2bf, a), __builtin_bit_cast(v2bf, b), c, false); }
DEVI u32 plo(u32 r0, u32 r1) { return __builtin_amdgcn_perm(r1, r0, 0x05040100u); }
DEVI u32 phi(u32 r0, u32 r1) { return __builtin_amdgcn_perm(r1, r0, 0x07060302u); }
DEVI u32 fu(float f) { return __builtin_bit_cast(u32, f); }
DEVI float uf(u32 u) { return __builtin_bit_cast(float, u); }
constexpr u32 SENT = 0x7f800001u;
template <int C> DEVI float dpp(float x) { return uf(__builtin_amdgcn_mov_dpp(fu(x), C, 0xf, 0xf, true)); }
constexpr int XOR1 = 0xB1, XOR2 = 0x4E, XOR7 = 0x141, XOR15 = 0x140, ROR8 = 0x128, ROR4 = 0x124;
DEVI float sw16(float a, float b) { auto s = __builtin_amdgcn_permlane16_swap(fu(a), fu(b), false, false); return uf(s[0]) + uf(s[1]); }
DEVI float sw32(float a, float b) { auto s = __builtin_amdgcn_permlane32_swap(fu(a), fu(b), false, false); return uf(s[0]) + uf(s[1]); }
DEVI float mx16(float a) { auto s = __builtin_amdgcn_permlane16_swap(fu(a), fu(a), false, false); return fmaxf(uf(s[0]), uf(s[1])); }
DEVI float mx32(float a) { auto s = __builtin_amdgcn_permlane32_swap(fu(a), fu(a), false, false); return fmaxf(uf(s[0]), uf(s[1])); }
DEVI float sum16(float x) { x += dpp<XOR1>(x); x += dpp<XOR2>(x); x += dpp<XOR7>(x); x += dpp<XOR15>(x); return x; }
DEVI float sum32(float x) { x = sum16(x); return sw16(x, x); }
DEVI float sum64(float x) { x = sum32(x); return sw32(x, x); }
DEVI float max32(float x) { x = fmaxf(x, dpp<XOR1>(x)); x = fmaxf(x, dpp<XOR2>(x)); x = fmaxf(x, dpp<XOR7>(x)); x = fmaxf(x, dpp<XOR15>(x)); return mx16(x); }
DEVI v4f mfma(v4u a, v4u b, v4f c) { return __builtin_amdgcn_mfma_f32_16x16x32_bf16(__builtin_bit_cast(v8bf, a), __builtin_bit_cast(v8bf, b), c, 0, 0, 0); }
template <class T> DEVI T ldw(const void* p) { return __builtin_nontemporal_load((const GAS T*)p); }
#define FENCE() asm volatile("" ::: "memory")
#define LGKM0() asm volatile("s_waitcnt lgkmcnt(0)" ::: "memory")
#define VM0() asm volatile("s_waitcnt vmcnt(0)" ::: "memory")
DEVI void wgbarrier() { LGKM0(); __builtin_amdgcn_s_barrier(); FENCE(); }
constexpr int SC1 = 16;
typedef __amdgpu_buffer_rsrc_t rsrc_t;
DEVI v4u ld128(rsrc_t r, int off) { return __builtin_amdgcn_raw_buffer_load_b128(r, off, 0, SC1); }
DEVI v2u ld64(rsrc_t r, int off) { return __builtin_amdgcn_raw_buffer_load_b64(r, off, 0, SC1); }
DEVI u32 ld32(rsrc_t r, int off) { return __builtin_amdgcn_raw_buffer_load_b32(r, off, 0, SC1); }
DEVI void st128(rsrc_t r, int off, v4u v) { __builtin_amdgcn_raw_buffer_store_b128(v, r, off, 0, SC1); }
DEVI void st64(rsrc_t r, int off, v2u v) { __builtin_amdgcn_raw_buffer_store_b64(v, r, off, 0, SC1); }
DEVI void st32(rsrc_t r, int off, u32 v) { __builtin_amdgcn_raw_buffer_store_b32(v, r, off, 0, SC1); }
DEVI bool full4(v4u v) { return (v.x != SENT) & (v.y != SENT) & (v.z != SENT) & (v.w != SENT); }
DEVI bool all4(v4u v, u32 t) { return (v.x == t) & (v.y == t) & (v.z == t) & (v.w == t); }
DEVI v4f f4(v4u v) { return __builtin_bit_cast(v4f, v); }
DEVI v4u u4(float a, float b, float c, float d) { return v4u{fu(a), fu(b), fu(c), fu(d)}; }
#define PIN8(a, i) asm volatile("" : "+v"(a[i]), "+v"(a[i + 1]), "+v"(a[i + 2]), "+v"(a[i + 3]), "+v"(a[i + 4]), "+v"(a[i + 5]), "+v"(a[i + 6]), "+v"(a[i + 7]))
#define SCHED() __builtin_amdgcn_sched_barrier(0)
DEVI u32 mktag(u32 e) { return e * 1664525u + 1013904223u; }
DEVI void lds_post_time(LAS unsigned char* L, int off) { FENCE(); *(volatile LAS u32*)(L + off) = (u32)__builtin_amdgcn_s_memrealtime() | 1u; }
DEVI u32 later(u32 a, u32 b) { return (int)(a - b) > 0 ? a : b; }
DEVI void sleep_until(u32 t) { while ((int)((u32)__builtin_amdgcn_s_memrealtime() - t) < 0) __builtin_amdgcn_s_sleep(1); }
DEVI int lds_word(LAS unsigned char* L, int off) { return __builtin_amdgcn_readfirstlane(*(volatile LAS int*)(L + off)); }
DEVI u32 lds_wait_time(LAS unsigned char* L, int off) { u32 v; while ((v = lds_word(L, off)) == 0) {} FENCE(); return v; }
template <int SL = 0> DEVI void lds_wait(LAS unsigned char* L, int off, int need) { while (lds_word(L, off) < need) { if (SL) __builtin_amdgcn_s_sleep(SL); } FENCE(); }
template <int SL = 0> DEVI bool poll_done(bool ok, u32& budget) { FENCE(); if (__all(ok)) return true; if (budget == 0) return true; --budget; if (SL) __builtin_amdgcn_s_sleep(SL); return false; }
DEVI float silu_acc(float x) {
  const float t = -x * LOG2E, e = fmaf(-x, LOG2E, -t) + -x * 1.925963033500011e-8f;
  const float p = __builtin_amdgcn_exp2f(fminf(t, 126.f)), d = 1.f + fmaf(p * e, 0.6931471805599453f, p);
  const float rc = __builtin_amdgcn_rcpf(d), q = x * rc;
  return fmaf(fmaf(-q, d, x), rc, q);
}
DEVI u64 rtime() { return __builtin_amdgcn_s_memrealtime(); }
#define STAMP(i) do {} while (0)
#define DUMP() do {} while (0)

DEVI void dma8(v4u d, u32 soff, u32 l, u32 a0, u32 a1, u32 a2, u32 a3) {
  u32 s2;
  asm volatile("s_mov_b32 m0, %2\n\ts_add_u32 %0, %3, 4096\n\t"
    "buffer_load_dwordx4 %4, %1, %3 offen offset:0 nt lds\n\tbuffer_load_dwordx4 %5, %1, %3 offen offset:1024 nt lds\n\t"
    "buffer_load_dwordx4 %6, %1, %3 offen offset:2048 nt lds\n\tbuffer_load_dwordx4 %7, %1, %3 offen offset:3072 nt lds\n\t"
    "s_add_u32 m0, %2, 4096\n\ts_nop 0\n\t"
    "buffer_load_dwordx4 %4, %1, %0 offen offset:0 nt lds\n\tbuffer_load_dwordx4 %5, %1, %0 offen offset:1024 nt lds\n\t"
    "buffer_load_dwordx4 %6, %1, %0 offen offset:2048 nt lds\n\tbuffer_load_dwordx4 %7, %1, %0 offen offset:3072 nt lds"
    : "=&s"(s2) : "s"(d), "s"(l), "s"(soff), "v"(a0), "v"(a1), "v"(a2), "v"(a3) : "memory", "scc");
}
DEVI v4u mksrd(const void* p, u32 bytes) { const size_t q = (size_t)p; return v4u{(u32)q, (u32)(q >> 32) & 0xffffu, bytes, 0x20000u}; }
#define PIECES(D, SO, A0, A1, A2, A3) \
    "buffer_load_dwordx4 " A0 ", " D ", " SO " offen offset:0 nt lds\n\tbuffer_load_dwordx4 " A1 ", " D ", " SO " offen offset:1024 nt lds\n\t" \
    "buffer_load_dwordx4 " A2 ", " D ", " SO " offen offset:2048 nt lds\n\tbuffer_load_dwordx4 " A3 ", " D ", " SO " offen offset:3072 nt lds\n\t"
#define STR2(x) #x
#define STR(x) STR2(x)
DEVI void stream(u32 evt, u32 sch, u32 gap, u32 nt, u32 so0, u32 t0, u32 ph, u32 wo, const void* kb, const void* vb,
                 u32 a0, u32 a1, u32 a2, u32 a3, u32 v0, u32 v1, u32 v2, u32 v3) {
  u32 u, sl, so, nxt, gmn, tl, s1, s2, va, vdn, vt, vf;
  asm volatile(
    "s_mov_b32 %[u], 0\n\ts_mov_b32 %[sl], 0\n\ts_mov_b32 %[so], %[so0]\n\ts_mov_b32 %[nxt], %[sch]\n\tv_mov_b32 %[vf], 148480\n\t"
    "s_mov_b64 s[92:93], %[kb]\n\ts_and_b32 s93, s93, 0xffff\n\ts_mov_b32 s94, 0x1000000\n\ts_mov_b32 s95, 0x20000\n\t"
    "s_lshl_b32 %[tl], %[nt], 1\n\ts_sub_u32 %[tl], %[tl], " STR(TAILU) "\n\ts_cmp_eq_u32 %[evt], 0\n\ts_cbranch_scc1 Lc%=\n"
    "Lgap%=:\n\t"
    "s_lshr_b32 %[s1], %[gap], " STR(HURRY) "\n\ts_sub_u32 %[gmn], %[gap], %[s1]\n"
    "Lunit%=:\n\t"
    "s_lshl_b32 %[s2], %[u], 1\n\ts_add_u32 %[s2], %[s2], %[wo]\n\ts_and_b32 %[s2], %[s2], 12\n\ts_add_u32 %[s2], %[s2], 148496\n\tv_mov_b32 %[va], %[s2]\n\ts_sub_u32 %[s1], %[u], 16\n\ts_mov_b32 m0, %[sl]\n"
    "Lw%=:\n\t"
    "s_memrealtime vcc\n\tds_read_b32 %[vdn], %[va]\n\ts_waitcnt lgkmcnt(0)\n\tv_readfirstlane_b32 %[s2], %[vdn]\n\t"
    "s_lshl_b32 vcc_lo, vcc_lo, 4\n\ts_sub_u32 vcc_hi, vcc_lo, %[nxt]\n\ts_cmp_lt_i32 vcc_hi, 0\n\ts_cbranch_scc1 Lw%=\n\t"
    "s_cmp_le_i32 %[s2], %[s1]\n\ts_cbranch_scc1 Lw%=\n\t"
    "s_add_u32 %[s2], %[so], 32768\n\t"
    PIECES("s[92:95]", "%[so]", "%[a0]", "%[a1]", "%[a2]", "%[a3]")
    "s_add_u32 m0, %[sl], 4096\n\ts_add_u32 %[sch], %[sch], %[gap]\n\t"
    PIECES("s[92:95]", "%[s2]", "%[a0]", "%[a1]", "%[a2]", "%[a3]")
    "s_sub_u32 vcc_hi, vcc_hi, " STR(SLACK) "\n\ts_max_i32 vcc_hi, vcc_hi, 0\n\ts_add_u32 %[nxt], %[nxt], vcc_hi\n\ts_add_u32 %[nxt], %[nxt], %[gmn]\n\ts_sub_u32 %[s1], %[nxt], %[sch]\n\ts_cmp_lt_i32 %[s1], 0\n\ts_cselect_b32 %[nxt], %[sch], %[nxt]\n\t"
    "s_add_u32 %[so], %[so], 77*65536\n\ts_and_b32 %[so], %[so], 0xffffff\n\ts_add_u32 %[sl], %[sl], 8192\n\ts_and_b32 %[sl], %[sl], 0x1ffff\n\t"
    "s_sub_u32 %[s1], %[u], " STR(LAGU) "-1\n\ts_cmp_ge_u32 %[u], %[tl]\n\ts_cbranch_scc1 Lt%=\n\t"
    "s_waitcnt vmcnt(8*" STR(LAGU) ")\n"
    "La%=:\n\t"
    "v_mov_b32 %[vt], %[s1]\n\ts_add_u32 %[u], %[u], 1\n\ts_cmp_lg_u32 %[u], %[evt]\n\tds_write_b32 %[vf], %[vt]\n\t"
    "s_cbranch_scc1 Lunit%=\n\t"
    "s_cmp_eq_u32 %[u], 2\n\ts_cbranch_scc0 Lb%=\n\t"
    "s_waitcnt lgkmcnt(0)\n\ts_barrier\n\ts_mov_b32 %[evt], " STR(NF0) "\n\ts_branch Lunit%=\n"
    "Lt%=:\n\t"
    "s_sub_u32 %[s1], %[u], 2\n\ts_waitcnt vmcnt(24)\n\ts_branch La%=\n"
    "Lb%=:\n\t"
    "s_add_u32 %[s1], %[tl], " STR(TAILU) "-116\n\ts_cmp_lt_u32 %[u], %[s1]\n\ts_cbranch_scc0 Lc%=\n\t"
    "s_mov_b32 %[evt], %[s1]\n\ts_mov_b32 %[sch], %[nxt]\n\ts_add_u32 %[s1], %[t0], " STR(TC) "\n\ts_lshl_b32 %[s1], %[s1], 4\n\ts_sub_u32 %[s1], %[s1], %[sch]\n\t"
    "s_mul_i32 %[s1], %[s1], 65536/(20-" STR(NF0) ")\n\ts_ashr_i32 %[s1], %[s1], 16\n\ts_max_i32 %[gap], %[s1], " STR(HMIN) "\n\ts_branch Lgap%=\n"
    "Lc%=:\n\t"
    "s_cmp_lt_u32 %[u], %[nt]\n\ts_cbranch_scc0 Ld%=\n\t"
    "s_add_u32 %[s1], %[tl], " STR(TAILU) "-116\n\ts_sub_u32 %[s1], %[u], %[s1]\n\ts_add_u32 %[evt], %[u], " STR(REAN) "\n\ts_cmp_eq_u32 %[s1], 0\n\ts_cselect_b32 %[evt], %[evt], %[nt]\n\t"
    "v_mov_b32 %[va], 148492\n\ts_mov_b32 %[gap], " STR(GAP) "\n\ts_mul_i32 %[sch], %[s1], " STR(GAP) "\n\ts_add_u32 %[sch], %[sch], %[ph]\n"
    "Le%=:\n\t"
    "ds_read_b32 %[vdn], %[va]\n\ts_waitcnt lgkmcnt(0)\n\tv_readfirstlane_b32 %[s2], %[vdn]\n\ts_nop 3\n\t"
    "s_cmp_lg_u32 %[s2], 0\n\ts_cbranch_scc1 Lf%=\n\ts_cmp_lg_u32 %[s1], 0\n\ts_cbranch_scc1 Le%=\n\ts_add_u32 %[s2], %[t0], " STR(TC) "\n"
    "Lf%=:\n\t"
    "s_lshl_b32 %[s2], %[s2], 4\n\ts_add_u32 %[sch], %[sch], %[s2]\n\t"
    "s_sub_u32 %[s1], %[nxt], %[sch]\n\ts_cmp_lt_i32 %[s1], 0\n\ts_cselect_b32 %[nxt], %[sch], %[nxt]\n\ts_branch Lgap%=\n"
    "Ld%=:\n\t"
    "s_cmp_eq_u32 %[u], %[nt]\n\ts_cbranch_scc0 Ldone%=\n\t"
    "s_mov_b64 s[92:93], %[vb]\n\ts_and_b32 s93, s93, 0xffff\n\t"
    "v_mov_b32 %[a0], %[v0]\n\tv_mov_b32 %[a1], %[v1]\n\tv_mov_b32 %[a2], %[v2]\n\tv_mov_b32 %[a3], %[v3]\n\t"
    "s_mov_b32 %[so], %[so0]\n\ts_lshl_b32 %[evt], %[nt], 1\n\ts_branch Lunit%=\n"
    "Ldone%=:"
    : [u] "=&s"(u), [sl] "=&s"(sl), [so] "=&s"(so), [nxt] "=&s"(nxt), [gmn] "=&s"(gmn), [tl] "=&s"(tl), [evt] "+&s"(evt), [sch] "+&s"(sch), [gap] "+&s"(gap),
      [s1] "=&s"(s1), [s2] "=&s"(s2), [va] "=&v"(va), [vdn] "=&v"(vdn), [vt] "=&v"(vt), [vf] "=&v"(vf),
      [a0] "+&v"(a0), [a1] "+&v"(a1), [a2] "+&v"(a2), [a3] "+&v"(a3)
    : [kb] "s"(kb), [vb] "s"(vb), [nt] "s"(nt), [so0] "s"(so0), [t0] "s"(t0), [ph] "s"(ph), [wo] "s"(wo),
      [v0] "v"(v0), [v1] "v"(v1), [v2] "v"(v2), [v3] "v"(v3)
    : "memory", "scc", "vcc", "s92", "s93", "s94", "s95");
}
static_assert(L_FULL == 148480 && L_TREF == 148492 && NF0 > 2 && NF0 < 20, "literals in stream()");
static_assert(L_DONE == 148496, "literal in stream_tiles");
template <int N> DEVI void waitvm() { asm volatile("s_waitcnt vmcnt(%0)" :: "n"(N) : "memory"); }

DEVI float reduce_cols(v4f s) {
  const float p0 = sw32(s.x, s.z), p1 = sw32(s.y, s.w);
  float t = sw16(p0, p1);
  t += dpp<ROR8>(t); t += dpp<ROR4>(t); t += dpp<XOR2>(t);
  return t;
}

template <int NP> DEVI v4f gather_sum(rsrc_t ws, int off, u32& budget) {
  for (;;) {
    v4u p[NP];
#pragma unroll
    for (int i = 0; i < NP; ++i) p[i] = __builtin_amdgcn_raw_buffer_load_b128(ws, off + (i & 3) * 1024, (i >> 2) * 4096, SC1);
    v4f s = ((f4(p[0]) + f4(p[1])) + (f4(p[2]) + f4(p[3]))) + ((f4(p[4]) + f4(p[5])) + (NP == 8 ? f4(p[6]) + f4(p[NP - 1]) : f4(p[6])));
    const float c = (s.x + s.y) + (s.z + s.w);
    FENCE();
    if (__all(c == c)) return s;
    bool ok = true;
#pragma unroll
    for (int i = 0; i < NP; ++i) ok &= full4(p[i]);
    if (poll_done<PSL>(ok, budget)) return s;
  }
}
DEVI void block_reduce(LAS unsigned char* L, float (&y)[8], int red, int cnt, int w, int lane) {
#pragma unroll
  for (int j = 0; j < 4; ++j) y[j] = sw32(y[j], y[j + 4]);
#pragma unroll
  for (int j = 0; j < 2; ++j) y[j] = sw16(y[j], y[j + 2]);
  y[0] += dpp<ROR8>(y[0]); y[1] += dpp<ROR8>(y[1]);
  if ((lane & 8) == 0) *(LAS v2f*)(L + red + (w * 64 + 8 * (lane & 7) + 4 * (lane >> 5) + 2 * ((lane >> 4) & 1)) * 4) = v2f{y[0], y[1]};
  LGKM0();
  if (lane == 0) __hip_atomic_fetch_add((LAS u32*)(L + cnt), 1u, __ATOMIC_RELAXED, __HIP_MEMORY_SCOPE_WORKGROUP);
}

}
extern "C" __global__ void __launch_bounds__(NTHR, 2) __attribute__((amdgpu_waves_per_eu(2, 2))) fused_decode_block(Args a) {
  __shared__ __attribute__((aligned(16))) unsigned char lds_raw[LDS_BYTES];
  LAS unsigned char* L = (LAS unsigned char*)lds_raw;
  volatile LAS u32* FULL = (volatile LAS u32*)(L + L_FULL);
  volatile LAS u32* DONE = (volatile LAS u32*)(L + L_DONE);
  const int lane = threadIdx.x & 63, wave = __builtin_amdgcn_readfirstlane(threadIdx.x >> 6);
  const int b = blockIdx.x, x = b & 7, r = b >> 3;
  const u32 t0 = (u32)rtime();
  auto warm_code = [&](int part, int parts) INL {
    u64 pc; u32 len;
    asm volatile("s_getpc_b64 %0\nLpc%=:\n\ts_mov_b32 %1, Lpc%=-" KNAME "\n\ts_sub_u32 %1, 0, %1" : "=s"(pc), "=s"(len) :: "scc");
    const char* base = (const char*)(pc + (int)len);
    u32 acc = 0;
    for (u32 o = (part * 64 + lane) * 128; o < CODE_BYTES; o += parts * 64 * 128) acc ^= *(const GAS u32*)(base + o);
    asm volatile("" :: "v"(acc));
  };
  const rsrc_t ws = __builtin_amdgcn_make_buffer_rsrc(a.ws, 0, W_END, 0x00020000);
  u32 budget = SPIN_CAP;
  const int grp = 4 * x + (r >> 3), u0 = 64 * grp + 8 * (r & 7);
  const bool ffn = grp < 28;
  const bool is_a = r < 4, is_mod = r < 8, is_q = (r >> 3) == 1, busy = r < 16;
  const int nt = is_q ? NTQ : is_mod ? NTM : NTF, nu = 2 * nt, wo = -nt & 3;
  const u32 L_WO = (nu & 15) * SLOT;

  if (wave == 4) {
    STAMP(0);
    __builtin_amdgcn_s_setprio(3);
    if (lane < 20) *(volatile LAS u32*)(L + L_FULL + lane * 4) = 0;
    const u32 g4 = lane >> 4, pos = lane & 15;
    const u32 rw = (g4 & 1) * 256 + (g4 >> 1) * 4096;
    const u32 k0 = rw + 16 * (pos ^ g4), k1 = (k0 ^ 64) + PSTR, k2 = (k0 ^ 128) + 2 * PSTR, k3 = (k0 ^ 192) + 3 * PSTR;
    const u32 v0 = rw + 16 * (pos ^ (g4 << 1)), v1 = (v0 ^ 128) + PSTR, v2 = v0 + 2 * PSTR, v3 = v1 + 2 * PSTR;
    const int j0 = is_mod ? 0 : is_q ? NTM : r < 24 ? NTM + NTQ : NTM + NTQ + NTF;
    const u32 lin = lane * 16, c0 = t0 * 16, so0 = (((j0 * 77 + 37 * x) & 255) << 16) | ((r & 7) << 9);
    const u32 ph = (r * 430) >> 5;
    u32 sch = c0 + TF0 * 16;
    if (busy) {
      const u32 e0 = ld32(ws, W_E);
      wgbarrier();
      if (is_q) {
        const u32 tg = mktag(__builtin_amdgcn_readfirstlane(e0));
        for (int k = 0; k < 3; ++k) {
          sleep_until(t0 + TT0 + 50 * k);
          const v2u tx = ld64(ws, W_T0 + (lane & 7) * 128);
          int dt = tx.x - t0;
          dt = max(dt, __builtin_amdgcn_mov_dpp(dt, XOR1, 0xf, 0xf, true)); dt = max(dt, __builtin_amdgcn_mov_dpp(dt, XOR2, 0xf, 0xf, true)); dt = max(dt, __builtin_amdgcn_mov_dpp(dt, XOR7, 0xf, 0xf, true));
          FENCE();
          if (__all(tx.y == tg && dt > -400 && dt < 400)) { *(volatile LAS u32*)(L + L_T0M) = max(dt, 0); break; }
        }
      }
      if (is_a | is_q) lds_wait<1>(L, L_GO, 1);
      else sleep_until(t0 + TMB);
      sch = (u32)rtime() * 16;
    }
    STAMP(8);
    stream(busy ? nu - 116 : 2, sch, busy ? max((int)(c0 + TC * 16 - sch) >> 3, HMIN) : HF0, nt, so0, t0, ph, wo * 4, a.k + (size_t)x * (SK * DH), a.v + (size_t)x * (SK * DH), k0, k1, k2, k3, v0, v1, v2, v3);
    const u32 tend = lds_wait_time(L, L_TEND);
    STAMP(1);
    waitvm<16>(); *FULL = nu - 2;
    waitvm<8>(); *FULL = nu - 1;
    lds_wait(L, L_DONE, nu - 14);
    const v4u od = mksrd(a.wo, 8 * D * D * 2);
    dma8(od, (512 * x + 16 * r) * (D * 2), L_WO, lin, lin, lin, lin); dma8(od, (512 * x + 16 * r + 8) * (D * 2), L_WO + 8192, lin, lin, lin, lin);
    waitvm<16>(); *FULL = nu;
    STAMP(2);
    waitvm<0>(); *(volatile LAS u32*)(L + L_WOK) = 1;
    STAMP(7);
    auto gather_x1 = [&](u32 when) INL {
      sleep_until(when);
      STAMP(13);
      v4u xa;
      do { xa = ld128(ws, W_X1 + lane * 64); } while (!poll_done<PSL>(full4(xa), budget));
      STAMP(11);
      *(LAS v4u*)(L + L_Q + lane * 16) = xa;
      const float sh = uf(__builtin_amdgcn_readfirstlane(xa.x) << 16);
      float s1 = 0.f, s2 = 0.f;
#pragma unroll
      for (int j = 0; j < 4; ++j) { const float d0 = bflo(xa[j]) - sh, d1 = bfhi(xa[j]) - sh; s1 += d0 + d1; s2 = fmaf(d0, d0, fmaf(d1, d1, s2)); }
      s1 = sum64(s1) * (1.f / D); s2 = sum64(s2) * (1.f / D);
      const float var = fmaxf(fmaf(-s1, s1, s2), 0.f);
      *(LAS v2f*)(L + L_STAT) = v2f{rb(sh + s1), __builtin_amdgcn_rcpf(rb(__builtin_amdgcn_sqrtf(rb(rb(var) + EPS))))};
      wgbarrier();
    };
    if (r >= 8) {
      if (ffn) gather_x1(later(lds_wait_time(L, L_PUB2) + D3, tend + CX));
      DUMP(); return;
    }
    const int rho = 8 * x + r, col = 8 * rho + 4 * (lane & 1) + 2 * (lane >> 5) + ((lane >> 4) & 1);
    const float xin = bflo(a.x[col]);
    const u32 tag = lds_word(L, L_TAG);
    v2u gga, ggb; v4u rd, r2;
    do { gga = ld64(ws, W_GM + ((2 * D + col) >> 1) * 8); ggb = ld64(ws, W_GM + ((5 * D + col) >> 1) * 8); rd = ld128(ws, W_RDY + lane * 16); r2 = ld128(ws, W_RDY + 1024 + lane * 16); }
    while (!poll_done<PSL>((gga.y == tag) & (ggb.y == tag) & all4(rd, tag) & all4(r2, tag), budget));
    const float ga = (col & 1) ? bfhi(gga.x) : bflo(gga.x), gb = (col & 1) ? bfhi(ggb.x) : bflo(ggb.x);
    sleep_until(later(lds_wait_time(L, L_PUB2) + D2, tend + C2));
    STAMP(12);
    v4f s = gather_sum<8>(ws, W_H2 + rho * 8192 + lane * 16, budget);
    STAMP(3);
    const float x1 = rb(xin + rb(ga * rb(reduce_cols(s))));
    { auto e = __builtin_amdgcn_permlane16_swap(fu(x1), fu(x1), false, false);
      const u32 pr = phi(e[0], e[1]);
      if ((lane & 30) == 0) st32(ws, W_X1 + rho * 64 + (col & 6) * 2, pr == SENT ? pr ^ 1u : pr); }
    const u32 tx = (u32)rtime();
    if (b == 0 && lane == 0) st32(ws, W_E, tag);
    STAMP(4);
    if (ffn) gather_x1(later(tx + DX, tend + CX));
    sleep_until(later(tx + D4, tend + C4));
    STAMP(14);
    s = gather_sum<7>(ws, W_H4 + rho * 7168 + lane * 16, budget);
    STAMP(5);
    const float y = x1 + rb(gb * rb(reduce_cols(s)));
    if ((lane & 14) == 0) a.out[col] = (u16)pk(y, y);
    STAMP(6); DUMP();
    return;
  }

  const int w = wave, t = threadIdx.x;
  STAMP(0);
  const int c16 = lane & 15, g = lane >> 4;
  const int row0 = 64 * (r & 7) + 16 * w + 4 * g;
  u32 gp[16], dp[32];
  auto ffn_weights = [&]() INL {
    v4u gr[4]; v2u dr[16];
#pragma unroll
    for (int j = 0; j < 4; ++j) gr[j] = ldw<v4u>((c16 & 8 ? a.wu : a.wg) + (size_t)(row0 + j) * F + 64 * grp + 8 * (c16 & 7));
    if (w == 2) {
#pragma unroll
      for (int j = 0; j < 16; ++j) dr[j] = ldw<v2u>(a.wd + (size_t)(u0 + (j >> 1)) * D + (j & 1) * 256 + 4 * lane);
    }
    FENCE();
    if (w == 2) {
#pragma unroll
      for (int j = 0; j < 4; ++j)
#pragma unroll
        for (int h = 0; h < 2; ++h) {
          const v2u r0 = dr[4 * j + h], r1 = dr[4 * j + 2 + h];
          dp[8 * j + 4 * h] = plo(r0.x, r1.x); dp[8 * j + 4 * h + 1] = phi(r0.x, r1.x); dp[8 * j + 4 * h + 2] = plo(r0.y, r1.y); dp[8 * j + 4 * h + 3] = phi(r0.y, r1.y);
        }
#pragma unroll
      for (int i = 0; i < 32; i += 8) PIN8(dp, i);
    }
#pragma unroll
    for (int j = 0; j < 4; ++j) { gp[2 * j] = plo(gr[0][j], gr[1][j]); gp[2 * j + 1] = phi(gr[0][j], gr[1][j]); gp[8 + 2 * j] = plo(gr[2][j], gr[3][j]); gp[9 + 2 * j] = phi(gr[2][j], gr[3][j]); }
    PIN8(gp, 0); PIN8(gp, 8);
  };
  u32 tag;
  if (busy) {
    const int mc = 4 * x + (r & 3), cb = is_mod ? 16 + mc : 8 * x + (r & 7), ld = is_mod ? 6 * D : 8 * D;
    if (is_a) {
      const u32 inA = *(const GAS u32*)(a.c + 256 * (mc & 1) + 64 * w + 2 * (lane & 31));
      const u32 e0 = ld32(ws, W_E);
      const u16* wp = a.wm + (size_t)(256 * (mc & 1) + 8 * (t >> 3)) * (6 * D) + 64 * (mc >> 1) + 8 * (t & 7);
      v4u wa[8];
#pragma unroll
      for (int i = 0; i < 8; ++i) wa[i] = ldw<v4u>(wp + i * (6 * D));
      FENCE();
      __builtin_amdgcn_s_barrier();
      FENCE();
      *(LAS u32*)(L + L_VECA + (w * 32 + (lane & 31)) * 4) = pk(silu_acc(bflo(inA)), silu_acc(bfhi(inA)));
      LGKM0();
      const v4u va = *(const LAS v4u*)(L + L_VECA + w * 128 + (lane >> 3) * 16);
      tag = mktag(__builtin_amdgcn_readfirstlane(e0));
      float y[8];
#pragma unroll
      for (int j = 0; j < 8; ++j) y[j] = 0.f;
#pragma unroll
      for (int i = 0; i < 4; ++i)
#pragma unroll
        for (int j = 0; j < 4; ++j) { y[2 * j] = dot2(plo(wa[2 * i][j], wa[2 * i + 1][j]), va[i], y[2 * j]); y[2 * j + 1] = dot2(phi(wa[2 * i][j], wa[2 * i + 1][j]), va[i], y[2 * j + 1]); }
      STAMP(1);
      block_reduce(L, y, L_REDA, L_CNTA, w, lane);
      if (w == 0) {
        lds_wait(L, L_CNTA, 4);
        if (lane < 32) {
          const v2f q2 = (*(LAS v2f*)(L + L_REDA + lane * 8) + *(LAS v2f*)(L + L_REDA + 256 + lane * 8)) + (*(LAS v2f*)(L + L_REDA + 512 + lane * 8) + *(LAS v2f*)(L + L_REDA + 768 + lane * 8));
          st128(ws, W_GP + (mc & 1) * 8192 + (mc >> 1) * 512 + lane * 16, v4u{fu(q2.x), tag, fu(q2.y), tag});
        }
        FENCE();
        *(volatile LAS u32*)(L + L_GO) = 1;
        STAMP(3);
      }
    } else {
    const u32 inB = *(const GAS u32*)((is_mod ? a.c : a.x) + 2 * t);
    const v4u xv = *(const GAS v4u*)(a.x + 8 * lane);
    const u32 e0 = ld32(ws, W_E);
    const u16* wp = (is_mod ? a.wm : a.wq) + (size_t)(16 * (t >> 3)) * ld + 64 * cb + 8 * (t & 7);
    v4u wr[16];
#pragma unroll
    for (int i = 0; i < 16; ++i) wr[i] = ldw<v4u>(wp + (size_t)i * ld);
    FENCE();
    __builtin_amdgcn_s_barrier();
    FENCE();
    tag = mktag(__builtin_amdgcn_readfirstlane(e0));
    u32 pair;
    if (is_mod) pair = pk(silu_acc(bflo(inB)), silu_acc(bfhi(inB)));
    else {
      float xs[8];
#pragma unroll
      for (int i = 0; i < 4; ++i) { xs[2 * i] = bflo(xv[i]); xs[2 * i + 1] = bfhi(xv[i]); }
      const float mean = sum64(((xs[0] + xs[1]) + (xs[2] + xs[3])) + ((xs[4] + xs[5]) + (xs[6] + xs[7]))) * (1.f / D);
      float q = 0.f;
#pragma unroll
      for (int i = 0; i < 8; ++i) q = fmaf(xs[i] - mean, xs[i] - mean, q);
      const float den = rb(sqrtf(rb(rb(sum64(q) * (1.f / D)) + EPS))), mb = rb(mean);
      pair = pk(rb(bflo(inB) - mb) / den, rb(bfhi(inB) - mb) / den);
    }
    u32 wp2[64];
#pragma unroll
    for (int i = 0; i < 8; ++i)
#pragma unroll
      for (int j = 0; j < 4; ++j) { wp2[8 * i + 2 * j] = plo(wr[2 * i][j], wr[2 * i + 1][j]); wp2[8 * i + 2 * j + 1] = phi(wr[2 * i][j], wr[2 * i + 1][j]); }
#pragma unroll
    for (int i = 0; i < 64; i += 8) PIN8(wp2, i);
    STAMP(1);
    if (!is_mod) {
      v4u s0, s1, c0_, c1;
      do { s0 = ld128(ws, W_GP + t * 16); s1 = ld128(ws, W_GP + 8192 + t * 16); c0_ = ld128(ws, W_GP + 4096 + t * 16); c1 = ld128(ws, W_GP + 12288 + t * 16); }
      while (!poll_done<PSL>((s0.y == tag) & (s0.w == tag) & (s1.y == tag) & (s1.w == tag) & (c0_.y == tag) & (c0_.w == tag) & (c1.y == tag) & (c1.w == tag), budget));
      if (w == 0) {
        const u32 tc = later((u32)rtime() + DTC, t0 + lds_word(L, L_T0M) + TC) | 1;
        *(volatile LAS u32*)(L + L_TREF) = tc;
        if (r == 11 && lane == 0) st64(ws, W_QS + x * 256, v2u{tc, tag});
      }
      const u32 sa = pk(uf(s0.x) + uf(s1.x), uf(s0.z) + uf(s1.z)), ca = pk(uf(c0_.x) + uf(c1.x), uf(c0_.z) + uf(c1.z));
      const u32 c1p = pk(1.f + bflo(ca), 1.f + bfhi(ca)), nc = pk(bflo(pair) * bflo(c1p), bfhi(pair) * bfhi(c1p));
      pair = pk(bflo(nc) + bflo(sa), bfhi(nc) + bfhi(sa));
      STAMP(2);
    }
    *(LAS u32*)(L + L_VECB + t * 4) = pair;
    LGKM0();
    const v4u va = *(const LAS v4u*)(L + L_VECB + (t >> 3) * 32), vb = *(const LAS v4u*)(L + L_VECB + (t >> 3) * 32 + 16);
    float y[8];
#pragma unroll
    for (int j = 0; j < 8; ++j) y[j] = 0.f;
#pragma unroll
    for (int i = 0; i < 8; ++i)
#pragma unroll
      for (int j = 0; j < 8; ++j) y[j] = dot2(wp2[8 * i + j], i < 4 ? va[i] : vb[i - 4], y[j]);
    block_reduce(L, y, L_REDB, L_CNTB, w, lane);
    if (w == 0) {
      lds_wait(L, L_CNTB, 4);
      if (lane < 16) {
        const v4f q4 = (*(LAS v4f*)(L + L_REDB + lane * 16) + *(LAS v4f*)(L + L_REDB + 256 + lane * 16)) + (*(LAS v4f*)(L + L_REDB + 512 + lane * 16) + *(LAS v4f*)(L + L_REDB + 768 + lane * 16));
        st128(ws, (is_mod ? W_GM : W_GQ) + cb * 256 + lane * 16, v4u{pk(q4.x, q4.y), tag, pk(q4.z, q4.w), tag});
      }
      FENCE();
      *(volatile LAS u32*)(L + L_GO) = 1;
      STAMP(3);
    }
    }
  } else {
    const u32 e0 = ld32(ws, W_E);
    if (WARM0 && r == 17) warm_code(w, 4);
    if (CLRAT == 1) {
#pragma unroll
      for (int k = 0; k < 4; ++k) __builtin_amdgcn_raw_buffer_store_b128(v4u{SENT, SENT, SENT, SENT}, ws, W_CLR + (b - 128) * 16384 + k * 4096 + t * 16, 0, CLRAUX);
    }
    FENCE();
    __builtin_amdgcn_s_barrier();
    FENCE();
    tag = mktag(__builtin_amdgcn_readfirstlane(e0));
    if (r == 16 && t == 0) st64(ws, W_T0 + x * 128, v2u{t0, tag});
    if (ffn && !FWL) { ffn_weights(); }
    if (CLRAT == 1) { VM0(); if (lane == 0) st32(ws, W_RDY + ((b - 128) * 4 + w) * 4, tag); }
  }
  if (t == 0) *(volatile LAS u32*)(L + L_TAG) = tag;
  if (ffn && (FWL ? is_mod : busy)) { ffn_weights(); }
  if (TOUCH && w == 3) {
    const u16* p = lane == 0 ? a.wo + (size_t)(512 * x + 16 * r) * D : lane == 1 ? a.wd + (ffn ? u0 * D : 0) : (lane & 1 ? a.wg : a.wu) + (64 * (r & 7) + (lane & 2 ? 63 : 0)) * F;
    u32 v0 = 0, v1 = ld32(ws, W_END - 4);
    if (lane < 6) v0 = ldw<u16>(p);
    asm volatile("" :: "v"(v0), "v"(v1));
  }

  u32 ka[4], va[8];
#pragma unroll
  for (int s = 0; s < 4; ++s) ka[s] = 256 * c16 + 16 * ((g ^ c16) ^ (4 * s));
  { const int q = (lane >> 2) & 3, p = lane & 3, row = 4 * g + q;
#pragma unroll
    for (int n = 0; n < 8; ++n) va[n] = row * 256 + 16 * ((2 * n + (p >> 1)) ^ ((row & 7) << 1)) + 8 * (p & 1); }
  auto wait_full = [&](int need) INL { lds_wait<WSL>(L, L_FULL, need); };
  auto slot_base = [&](int u) INL -> u32 { return (u & 15) * SLOT; };
  auto read_k = [&](v4u (&k)[8], int u) INL {
    const u32 base = slot_base(u);
    wait_full(u + 1);
#pragma unroll
    for (int s = 0; s < 4; ++s) { k[s] = *(const LAS v4u*)(L + base + ka[s]); k[4 + s] = *(const LAS v4u*)(L + base + ka[s] + 4096); }
    LGKM0();
    DONE[w] = u + 1;
  };
  v4u qf[4];
  float mrun = -INFINITY;
  const u32 sa = L_S + (g * 4 + (c16 & 3)) * 16;
  auto qk = [&](v4u (&k)[8], int n) INL {
    v4f s0 = {0.f, 0.f, 0.f, 0.f}, s1 = {0.f, 0.f, 0.f, 0.f};
#pragma unroll
    for (int s = 0; s < 4; ++s) { s0 = mfma(k[s], qf[s], s0); s1 = mfma(k[4 + s], qf[s], s1); }
    v4u sc;
#pragma unroll
    for (int i = 0; i < 4; ++i) {
      const u32 raw = i < 2 ? pk(s0[2 * i], s0[2 * i + 1]) : pk(s1[2 * i - 4], s1[2 * i - 3]);
      sc[i] = pk(bflo(raw) * SCALE, bfhi(raw) * SCALE);
      mrun = fmaxf(mrun, fmaxf(bflo(sc[i]), bfhi(sc[i])));
    }
    if (c16 < 4) *(LAS v4u*)(L + sa + n * 256) = sc;
  };
  {
    v4u k0[8], k1[8], k2[8], k3[8];
    const int n0 = 2 * ((w - wo) & 3);
    auto get_q = [&]() INL {
      sleep_until(t0 + TQ0);
      v2u qs;
      do { qs = ld64(ws, W_QS + x * 256); } while (!poll_done<QSL>(qs.y == tag, budget));
      if (!is_q) *(volatile LAS u32*)(L + L_TREF) = (qs.y == tag ? qs.x : t0) | 1;
      const int oq = W_GQ + x * 2048 + lane * 16;
      v4u g0, g1;
      do { g0 = ld128(ws, oq); g1 = ld128(ws, oq + 1024); } while (!poll_done<QSL>((g0.y == tag) & (g0.w == tag) & (g1.y == tag) & (g1.w == tag), budget));
      *(LAS v2u*)(L + L_Q + lane * 8) = v2u{g0.x, g0.z}; *(LAS v2u*)(L + L_Q + 512 + lane * 8) = v2u{g1.x, g1.z};
      LGKM0();
      *(volatile LAS u32*)(L + L_QOK) = 1;
    };
    if (w == 0 && busy) get_q();
    read_k(k0, n0); read_k(k1, n0 + 1); read_k(k2, n0 + 8); read_k(k3, n0 + 9);
    STAMP(4);
    if (w == 0 && !busy) get_q();
    lds_wait<1>(L, L_QOK, 1);
    FENCE();
#pragma unroll
    for (int s = 0; s < 4; ++s) qf[s] = *(const LAS v4u*)(L + L_Q + (c16 & 3) * 256 + s * 64 + g * 16);
    STAMP(5);
#pragma nounroll
    for (int j = 0; j < 32; j += 16) {
      qk(k0, j + n0); if (j == 0) read_k(k0, n0 + 16);
      qk(k1, j + n0 + 1); if (j == 0) read_k(k1, n0 + 17);
      qk(k2, j + n0 + 8); if (j == 0) read_k(k2, n0 + 24);
      qk(k3, j + n0 + 9); if (j == 0) read_k(k3, n0 + 25);
    }
#pragma nounroll
    for (int n = n0 + 32; n < nt; n += (n & 1) ? 7 : 1) {
      read_k(k0, n);
      qk(k0, n);
    }
  }
  STAMP(6);
  mrun = mx32(mx16(mrun));
  if (lane < 4) *(LAS float*)(L + L_MX + (lane * 4 + w) * 4) = mrun;
  LGKM0();
  if (lane == 0) __hip_atomic_fetch_add((LAS u32*)(L + L_CNTM), 1u, __ATOMIC_RELAXED, __HIP_MEMORY_SCOPE_WORKGROUP);
  lds_wait<1>(L, L_CNTM, 4);
  {
    const v4f m4 = *(const LAS v4f*)(L + L_MX + (c16 & 3) * 16);
    const float M = fmaxf(fmaxf(m4.x, m4.y), fmaxf(m4.z, m4.w)), ms = M == -INFINITY ? 0.f : M;
    float l = 0.f;
#pragma nounroll
    for (int n = 4 * w; n < nt; n += 16) {
      const u32 at = sa + (n + (c16 >> 2)) * 256;
      if (n + (c16 >> 2) >= nt) continue;
      const v4u sc = *(const LAS v4u*)(L + at);
      v4u p;
#pragma unroll
      for (int j = 0; j < 4; ++j) {
        const float p0 = __builtin_amdgcn_exp2f((bflo(sc[j]) - ms) * LOG2E), p1 = __builtin_amdgcn_exp2f((bfhi(sc[j]) - ms) * LOG2E);
        l += p0 + p1; p[j] = pk(p0, p1);
      }
      *(LAS v4u*)(L + at) = p;
    }
    l += dpp<ROR8>(l); l += dpp<ROR4>(l); l = sw16(l, l); l = sw32(l, l);
    if (lane < 4) *(LAS float*)(L + L_LS + (lane * 4 + w) * 4) = l;
    LGKM0();
    if (lane == 0) __hip_atomic_fetch_add((LAS u32*)(L + L_CNTL), 1u, __ATOMIC_RELAXED, __HIP_MEMORY_SCOPE_WORKGROUP);
    lds_wait<1>(L, L_CNTL, 4);
    if (w == 3) {
      if (lane < 4) { const v4f l4 = *(const LAS v4f*)(L + L_LS + lane * 16);
        st128(ws, W_ML + ((4 * x + lane) * 32 + r) * 16, v4u{fu(M), tag, fu((l4.x + l4.y) + (l4.z + l4.w)), tag}); }
    }
  }
  STAMP(12);
  v4f acc[8];
#pragma unroll
  for (int n = 0; n < 8; ++n) acc[n] = v4f{0.f, 0.f, 0.f, 0.f};
  const u32 ma = (c16 & 3) * 528 + g * 16;
  auto vtile = [&](int u) INL {
    v4u pf = *(const LAS v4u*)(L + sa + (u - nt) * 256);
    const u32 base = slot_base(u);
    u32 at[8];
#pragma unroll
    for (int k = 0; k < 8; ++k) at[k] = base + va[k];
    asm volatile("" : "+v"(pf)); PIN8(at, 0);
    wait_full(u + 1);
    v4u vf[8];
#pragma unroll
    for (int k = 0; k < 8; ++k) {
      const v2u lo = __builtin_bit_cast(v2u, __builtin_amdgcn_ds_read_tr16_b64_v4i16((LAS v4s*)(L + at[k])));
      const v2u hi = __builtin_bit_cast(v2u, __builtin_amdgcn_ds_read_tr16_b64_v4i16((LAS v4s*)(L + at[k] + 4096)));
      vf[k] = v4u{lo.x, lo.y, hi.x, hi.y};
    }
    SCHED();
#pragma unroll
    for (int k = 0; k < 8; ++k) acc[k] = mfma(vf[k], pf, acc[k]);
    SCHED();
    LGKM0();
    DONE[w] = u + 1;
  };
  int u = nt + 2 * ((w - wo - (nt >> 1)) & 3);
  auto vpairs = [&](int end) INL {
#pragma nounroll
    for (; u < end; u += (u & 1) ? 7 : 1) vtile(u);
  };
  vpairs(nt + 16);
  u32 sb0, sb1, cb0, cb1;
  {
    v4u gs, gc, rd, r2;
    do { gs = ld128(ws, W_GM + ((3 * D + row0) >> 1) * 8); gc = ld128(ws, W_GM + ((4 * D + row0) >> 1) * 8); rd = ld128(ws, W_RDY + lane * 16); r2 = ld128(ws, W_RDY + 1024 + lane * 16); }
    while (!poll_done<QSLEEP>((gs.y == tag) & (gs.w == tag) & (gc.y == tag) & (gc.w == tag) & all4(rd, tag) & all4(r2, tag), budget));
    sb0 = gs.x; sb1 = gs.z; cb0 = pk(1.f + bflo(gc.x), 1.f + bfhi(gc.x)); cb1 = pk(1.f + bflo(gc.z), 1.f + bfhi(gc.z));
  }
  if (w == 1) {
    const v2u qs = ld64(ws, W_QS + (lane & 7) * 256);
    int dt = qs.y == tag ? qs.x - t0 : 0;
    dt = max(dt, __builtin_amdgcn_mov_dpp(dt, XOR1, 0xf, 0xf, true)); dt = max(dt, __builtin_amdgcn_mov_dpp(dt, XOR2, 0xf, 0xf, true)); dt = max(dt, __builtin_amdgcn_mov_dpp(dt, XOR7, 0xf, 0xf, true));
    *(volatile LAS u32*)(L + L_TEND) = (t0 + min(dt, 2000) + TRUN) | 1;
  }
  vpairs(nt + 32);
  float wgt = 0.f;
  if (w == 0) {
    v4u ml;
    do { ml = ld128(ws, W_ML + ((4 * x + (r >> 3)) * 32 + (lane & 31)) * 16); } while (!poll_done<QSLEEP>((ml.y == tag) & (ml.w == tag), budget));
    const float M = max32(uf(ml.x)), e = __builtin_amdgcn_exp2f((uf(ml.x) - (M == -INFINITY ? 0.f : M)) * LOG2E);
    wgt = e / sum32(e * uf(ml.z));
  }
  const u32 tend = lds_wait_time(L, L_TEND);
  if (WARM) { vpairs(nu - WARM); if (w == 1 && r == 20) warm_code(0, 1); }
  vpairs(nu - 2);
  STAMP(15);
  const int ord = w;
  if (w < 3) {
    if (c16 < 4) {
#pragma unroll
      for (int n = 0; n < 8; ++n) *(LAS v4f*)(L + slot_base(nu - 7 + 2 * w) + ma + n * 64) = acc[n];
    }
    LGKM0();
    *(volatile LAS u32*)(L + L_MRG + w * 4) = 1;
    STAMP(7);
  } else {
    v4f o[8];
    auto fetch = [&](int v) INL {
      lds_wait(L, L_MRG + v * 4, 1);
      const u32 mb = slot_base(nu - 7 + 2 * v) + ma;
#pragma unroll
      for (int k = 0; k < 8; ++k) o[k] = *(const LAS v4f*)(L + mb + k * 64);
      SCHED();
    };
    auto add = [&]() INL {
#pragma unroll
      for (int k = 0; k < 8; ++k) acc[k] += o[k];
    };
    int oh1[4];
#pragma unroll
    for (int k = 0; k < 4; ++k) oh1[k] = W_H1 + ((x * 32 + 8 * (c16 & 3)) * 32 + r) * 64 + g * 16 + k * 4096;
    asm volatile("" : "+v"(oh1[0]), "+v"(oh1[1]), "+v"(oh1[2]), "+v"(oh1[3]));
    fetch(0); add(); PIN8(acc, 0);
    STAMP(1);
    fetch(1); add(); PIN8(acc, 0);
    STAMP(2);
    vtile(nu - 2);
    STAMP(3);
    fetch(2); PIN8(o, 0);
    STAMP(4);
    vtile(nu - 1);
    add();
    STAMP(13);
    if (c16 < 4) {
#pragma unroll
      for (int n = 0; n < 8; ++n) st128(ws, oh1[n >> 1] + (n & 1) * 2048, __builtin_bit_cast(v4u, acc[n]));
    }
    lds_post_time(L, L_PUB);
    STAMP(7);
  }
  const int kh = lane >> 5;
  if (ord == 0) {
    const int o1 = W_H1 + ((x * 32 + r) * 32 + (lane & 31)) * 64 + kh * 32;
    int o3 = W_H2 + (lane >> 1) * 8192 + b * 32 + (lane & 1) * 16, o3b = o3 + 32 * 8192;
    float y[8];
#pragma unroll
    for (int k = 0; k < 8; ++k) y[k] = 0.f;
    asm volatile("" : "+v"(o3), "+v"(o3b)); PIN8(y, 0);
    sleep_until(later(lds_wait_time(L, L_PUB) + D1 + (((31 - r) * GAP) >> 9), tend + C1));
    STAMP(10);
    v4u p0 = ld128(ws, o1), p1 = ld128(ws, o1 + 16);
    lds_wait(L, L_WOK, 1);
    v2u wr[32];
#pragma unroll
    for (int j = 0; j < 32; ++j) wr[j] = *(const LAS v2u*)(L + L_WO + j * 512 + lane * 8);
    SCHED();
    u32 wop[64];
#pragma unroll
    for (int j = 0; j < 8; ++j)
#pragma unroll
      for (int h = 0; h < 2; ++h) {
        const v2u r0 = wr[4 * j + h], r1 = wr[4 * j + 2 + h];
        wop[8 * j + 4 * h] = plo(r0.x, r1.x); wop[8 * j + 4 * h + 1] = phi(r0.x, r1.x); wop[8 * j + 4 * h + 2] = plo(r0.y, r1.y); wop[8 * j + 4 * h + 3] = phi(r0.y, r1.y);
      }
#pragma unroll
    for (int i = 0; i < 64; i += 8) PIN8(wop, i);
    SCHED();
    for (;;) {
      const v4f s = f4(p0) + f4(p1); const float c = (s.x + s.y) + (s.z + s.w);
      FENCE();
      if (__all(c == c)) break;
      if (poll_done<PSL>(full4(p0) & full4(p1), budget)) break;
      p0 = ld128(ws, o1); p1 = ld128(ws, o1 + 16);
    }
    STAMP(8);
    float ov[4];
#pragma unroll
    for (int j = 0; j < 4; ++j) ov[j] = sw16(wgt * uf(p0[j]), wgt * uf(p1[j]));
#pragma unroll
    for (int j = 0; j < 2; ++j) { const float p = ov[j] + dpp<ROR8>(ov[j]), q2 = ov[j + 2] + dpp<ROR8>(ov[j + 2]); ov[j] = (lane & 8) ? q2 : p; }
    ov[0] += dpp<XOR7>(ov[0]); ov[1] += dpp<XOR7>(ov[1]); ov[0] += dpp<XOR2>(ov[0]); ov[1] += dpp<XOR2>(ov[1]); ov[0] += dpp<XOR1>(ov[0]); ov[1] += dpp<XOR1>(ov[1]);
    const u32 op = pk(ov[0], ov[1]);
#pragma unroll
    for (int j = 0; j < 8; ++j) {
      const u32 oj = __builtin_amdgcn_readlane(op, 8 * j);
#pragma unroll
      for (int k = 0; k < 8; ++k) y[k] = dot2(oj, wop[8 * j + k], y[k]);
    }
    st128(ws, o3, u4(y[0], y[1], y[2], y[3]));
    st128(ws, o3b, u4(y[4], y[5], y[6], y[7]));
    lds_post_time(L, L_PUB2);
    STAMP(9);
  }
  if (!ffn) { DUMP(); return; }
  if (FWL && !is_mod) { lds_wait_time(L, L_PUB2); ffn_weights(); }
  float v[8], y[8];
#pragma unroll
  for (int j = 0; j < 8; ++j) v[j] = y[j] = 0.f;
  int o4 = W_H3 + ((8 * grp + (c16 & 7)) * 32 + 4 * (r & 7) + w) * 64 + (c16 >> 3) * 32 + g * 8;
  PIN8(v, 0); PIN8(y, 0); asm volatile("" : "+v"(o4));
  wgbarrier();
  {
    const v2u xo = *(const LAS v2u*)(L + L_Q + row0 * 2);
    const v2f stat = *(const LAS v2f*)(L + L_STAT);
    auto modulate = [&](u32 xp, u32 cbp, u32 sbp) INL {
      const u32 d2 = pk(bflo(xp) - stat.x, bfhi(xp) - stat.x), n2 = pk(bflo(d2) * stat.y, bfhi(d2) * stat.y), m2 = pk(bflo(n2) * bflo(cbp), bfhi(n2) * bfhi(cbp));
      return pk(bflo(m2) + bflo(sbp), bfhi(m2) + bfhi(sbp)); };
    const u32 h0 = modulate(xo.x, cb0, sb0), h1 = modulate(xo.y, cb1, sb1);
#pragma unroll
    for (int j = 0; j < 8; ++j) v[j] = dot2(gp[8 + j], h1, dot2(gp[j], h0, v[j]));
#pragma unroll
    for (int j = 0; j < 4; ++j) v[j] = sw32(v[j], v[j + 4]);
    st64(ws, o4, v2u{fu(sw16(v[0], v[2])), fu(sw16(v[1], v[3]))});
    STAMP(11);
  }
  if (ord != 2) { DUMP(); return; }
  {
    const int o1 = W_H3 + ((u0 >> 3) * 32 + (lane & 31)) * 64 + kh * 32;
    int o3 = W_H4 + (lane >> 1) * 7168 + (u0 >> 3) * 32 + (lane & 1) * 16, o3b = o3 + 32 * 7168;
    asm volatile("" : "+v"(o3), "+v"(o3b));
    sleep_until(later((u32)rtime() + D5, tend + C3));
    STAMP(10);
    v4u p0, p1;
    for (;;) {
      p0 = ld128(ws, o1); p1 = ld128(ws, o1 + 16);
      const v4f s = f4(p0) + f4(p1); const float c = (s.x + s.y) + (s.z + s.w);
      FENCE();
      if (__all(c == c)) break;
      if (poll_done<PSL>(full4(p0) & full4(p1), budget)) break;
    }
    STAMP(8);
    float ov[4];
#pragma unroll
    for (int j = 0; j < 4; ++j) ov[j] = sw16(uf(p0[j]), uf(p1[j]));
#pragma unroll
    for (int j = 0; j < 2; ++j) { const float p = ov[j] + dpp<ROR8>(ov[j]), q2 = ov[j + 2] + dpp<ROR8>(ov[j + 2]); ov[j] = (lane & 8) ? q2 : p; }
    ov[0] += dpp<XOR7>(ov[0]); ov[1] += dpp<XOR7>(ov[1]); ov[0] += dpp<XOR2>(ov[0]); ov[1] += dpp<XOR2>(ov[1]); ov[0] += dpp<XOR1>(ov[0]); ov[1] += dpp<XOR1>(ov[1]);
    const u32 gu = pk(ov[0], ov[1]);
    auto e2 = __builtin_amdgcn_permlane32_swap(gu, gu, false, false);
    const float g0 = bflo(e2[0]), g1 = bfhi(e2[0]);
    const u32 sg = pk(g0 * __builtin_amdgcn_rcpf(1.f + __builtin_amdgcn_exp2f(-g0 * LOG2E)), g1 * __builtin_amdgcn_rcpf(1.f + __builtin_amdgcn_exp2f(-g1 * LOG2E)));
    const u32 act = pk(bflo(sg) * bflo(e2[1]), bfhi(sg) * bfhi(e2[1]));
#pragma unroll
    for (int j = 0; j < 4; ++j) {
      const u32 aj = __builtin_amdgcn_readlane(act, 8 * j);
#pragma unroll
      for (int k = 0; k < 8; ++k) y[k] = dot2(aj, dp[8 * j + k], y[k]);
    }
    st128(ws, o3, u4(y[0], y[1], y[2], y[3]));
    st128(ws, o3b, u4(y[4], y[5], y[6], y[7]));
    STAMP(9); DUMP();
  }
}

extern "C" void kernel_launch(void* const* d_in, const int* in_sizes, int n_in, void* d_out, int out_size, void* d_ws, size_t ws_size, hipStream_t stream) {
  Args a;
  a.x = (const u16*)d_in[0]; a.k = (const u16*)d_in[1]; a.v = (const u16*)d_in[2]; a.wq = (const u16*)d_in[3]; a.wo = (const u16*)d_in[4];
  a.wg = (const u16*)d_in[6]; a.wu = (const u16*)d_in[7]; a.wd = (const u16*)d_in[8]; a.c = (const u16*)d_in[9]; a.wm = (const u16*)d_in[10];
  a.out = (u16*)d_out; a.ws = (unsigned char*)d_ws;
  a.ws += (ws_size - (4u << 20)) & ~(size_t)4095;
  hipLaunchKernelGGL(fused_decode_block, dim3(NBLK), dim3(NTHR), 0, stream, a);
}
```
